```python
import torch

def _softmax(x, axis=-1):
    return torch.softmax(x, dim=axis)

def _gelu(x):
    return torch.nn.functional.gelu(x, approximate='tanh')

def _silu(x):
    return torch.nn.functional.silu(x)

def _sigmoid(x):
    return torch.sigmoid(x)

def _layernorm(x, axis=-1, eps=1e-05):
    m = x.mean(dim=axis, keepdim=True)
    v = x.var(dim=axis, keepdim=True, unbiased=False)
    return (x - m) / torch.sqrt(v + eps)

def _scatter_add(tgt, idx, src, axis=0):
    return tgt.clone().index_add_(axis, idx, src)

def _segment_sum(src, seg, n):
    out = torch.zeros((n,) + src.shape[1:], dtype=src.dtype)
    return out.index_add_(0, seg, src)

def _pad(x, pw):
    flat = tuple((v for pair in reversed(pw) for v in pair))
    return torch.nn.functional.pad(x, flat)
kernel_name = 'standalone__kv_cache_quant_pre_rope_vs_post_rope__sampling'

def setup_inputs():

    _w = lambda a: (lambda t: t.to(torch.bfloat16) if t.is_floating_point() else t)(torch.as_tensor(a))
    B, Sq, Skv = (8, 1, 8192)
    H, Dh0 = (8, 128)
    G = -1 if -1 > 0 else Dh0
    G = max(2, G // 2 * 2)
    Dh = max(G, Dh0 // G * G)
    Skv = max(Skv, Sq)
    ng = Dh // G
    bits = 4 if 4 > 1 else 4
    bits = max(2, min(8, bits))
    qmax = (1 << bits) - 1
    half = Dh // 2
    inv = 1.0 / 10000.0 ** (torch.arange(half) / max(1, half))
    ang_k = torch.arange(Skv)[:, None] * inv[None, :]
    ang_q = torch.arange(Skv - Sq, Skv)[:, None] * inv[None, :]
    kc = torch.cat([torch.cos(ang_k), torch.cos(ang_k)], dim=-1)
    ks = torch.cat([torch.sin(ang_k), torch.sin(ang_k)], dim=-1)
    qc = torch.cat([torch.cos(ang_q), torch.cos(ang_q)], dim=-1)
    qs = torch.cat([torch.sin(ang_q), torch.sin(ang_q)], dim=-1)
    qk_i = torch.randint(0, qmax + 1, (B, H, Skv, Dh)).to(torch.int32)
    qv_i = torch.randint(0, qmax + 1, (B, H, Skv, Dh)).to(torch.int32)
    ksc = torch.abs((lambda *s: torch.randn(*s, dtype=torch.bfloat16) * 0.2)(B, H, Skv, ng)) * 0.1 + 0.01
    kzp = torch.full((B, H, Skv, ng), qmax / 2.0)
    vsc = torch.abs((lambda *s: torch.randn(*s, dtype=torch.bfloat16) * 0.2)(B, H, Skv, ng)) * 0.1 + 0.01
    vzp = torch.full((B, H, Skv, ng), qmax / 2.0)
    return {'q': _w((lambda *s: torch.randn(*s, dtype=torch.bfloat16) * 6.4)(B, H, Sq, Dh) * 0.5), 'qk': _w(qk_i), 'k_scales': _w(ksc), 'k_zeros': _w(kzp), 'qv': _w(qv_i), 'v_scales': _w(vsc), 'v_zeros': _w(vzp), 'q_cos': _w(qc), 'q_sin': _w(qs), 'k_cos': _w(kc), 'k_sin': _w(ks)}

def reference(q, qk, k_scales, k_zeros, qv, v_scales, v_zeros, q_cos, q_sin, k_cos, k_sin):
    B, H, Sq, Dh = q.shape
    ng = k_scales.shape[-1]
    G = Dh // ng
    half = Dh // 2
    ks = (lambda x, n, axis: torch.repeat_interleave(x, n, dim=axis))(k_scales, G, axis=-1)
    kz = (lambda x, n, axis: torch.repeat_interleave(x, n, dim=axis))(k_zeros, G, axis=-1)
    k = ((lambda x, dt: x.to(dt))(qk, q.dtype) - kz) * ks
    vs = (lambda x, n, axis: torch.repeat_interleave(x, n, dim=axis))(v_scales, G, axis=-1)
    vz = (lambda x, n, axis: torch.repeat_interleave(x, n, dim=axis))(v_zeros, G, axis=-1)
    v = ((lambda x, dt: x.to(dt))(qv, q.dtype) - vz) * vs
    k1 = k[..., :half].clone()
    k2 = k[..., half:].clone()
    krh = torch.concatenate([-k2, k1], axis=-1)
    kr = k * k_cos[None, None, :, :] + krh * k_sin[None, None, :, :]
    q1 = q[..., :half].clone()
    q2 = q[..., half:].clone()
    qrh = torch.concatenate([-q2, q1], axis=-1)
    qr = q * q_cos[None, None, :, :] + qrh * q_sin[None, None, :, :]
    logits = torch.einsum('bhqd,bhkd->bhqk', qr, kr) * float(Dh) ** (-0.5)
    attn = _softmax(logits, axis=-1)
    out = torch.einsum('bhqk,bhkd->bhqd', attn, v)
    return out
```

```cpp
#include <hip/hip_runtime.h>
#include <hip/hip_bf16.h>
#include <cstdint>

namespace {

constexpr int NB = 8, NH = 8, SKV = 8192, DH = 128;
constexpr int NHEAD = NB * NH;
constexpr int CHUNK = 256;
constexpr int NSPLIT = SKV / CHUNK;
constexpr int WAVES = 8;
constexpr int NTHR = WAVES * 64;
constexpr int NWG = NSPLIT * NHEAD / WAVES;
#define DEPTH 4
constexpr int D = DEPTH;
constexpr int PREC = 132;

using u32x4 = __attribute__((ext_vector_type(4))) unsigned;
using i32x4 = __attribute__((ext_vector_type(4))) int;
using f32x2 = __attribute__((ext_vector_type(2))) float;
using f32x4 = __attribute__((ext_vector_type(4))) float;
using bf16x2 = __attribute__((ext_vector_type(2))) __bf16;

__device__ __forceinline__ float bf2f(unsigned short x) { return __uint_as_float(((unsigned)x) << 16); }
__device__ __forceinline__ unsigned short f2bf(float x) { return __builtin_bit_cast(unsigned short, (__bf16)x); }
__device__ __forceinline__ float rbf(float x) { return bf2f(f2bf(x)); }
__device__ __forceinline__ float dot2(unsigned a, unsigned b) {
  return __builtin_amdgcn_fdot2_f32_bf16(__builtin_bit_cast(bf16x2, a), __builtin_bit_cast(bf16x2, b), 0.0f, false);
}
template <int CTRL> __device__ __forceinline__ float dpp(float x) {
  return __builtin_bit_cast(float, __builtin_amdgcn_mov_dpp(__builtin_bit_cast(int, x), CTRL, 0xf, 0xf, true));
}
constexpr int XOR1 = 0xB1, XOR2 = 0x4E, HMIR = 0x141, ROR8 = 0x128;
__device__ __forceinline__ float sum8(float x) {
  x += dpp<XOR1>(x); x += dpp<XOR2>(x); x += dpp<HMIR>(x); return x;
}
__device__ __forceinline__ float max8(float x) {
  x = fmaxf(x, dpp<XOR1>(x)); x = fmaxf(x, dpp<XOR2>(x)); x = fmaxf(x, dpp<HMIR>(x)); return x;
}
__device__ __forceinline__ float sum_groups(float x) {
  x += dpp<ROR8>(x);
  auto s = __builtin_amdgcn_permlane16_swap(__float_as_uint(x), __float_as_uint(x), false, false);
  x = __uint_as_float(s[0]) + __uint_as_float(s[1]);
  auto t = __builtin_amdgcn_permlane32_swap(__float_as_uint(x), __float_as_uint(x), false, false);
  return __uint_as_float(t[0]) + __uint_as_float(t[1]);
}
__device__ __forceinline__ float max_groups(float x) {
  x = fmaxf(x, dpp<ROR8>(x));
  auto s = __builtin_amdgcn_permlane16_swap(__float_as_uint(x), __float_as_uint(x), false, false);
  x = fmaxf(__uint_as_float(s[0]), __uint_as_float(s[1]));
  auto t = __builtin_amdgcn_permlane32_swap(__float_as_uint(x), __float_as_uint(x), false, false);
  return fmaxf(__uint_as_float(t[0]), __uint_as_float(t[1]));
}
template <int C> __device__ __forceinline__ float bcast8(float x) {
  constexpr int q = C & 3, QP = q | (q << 2) | (q << 4) | (q << 6);
  const int a = __builtin_amdgcn_mov_dpp(__builtin_bit_cast(int, x), QP, 0xf, 0xf, true);
  return __builtin_bit_cast(float, __builtin_amdgcn_update_dpp(a, a, HMIR, 0xf, C < 4 ? 0xa : 0x5, false));
}

struct Step { i32x4 v[4]; };

__device__ __forceinline__ void issue(Step& s, const char* p, unsigned voff) {
#pragma unroll
  for (int j = 0; j < 4; ++j) s.v[j] = __builtin_nontemporal_load(reinterpret_cast<const i32x4*>(p + voff + j * 128));
}

template <int TT> __device__ __forceinline__ void refill(Step& s, const char* cur, const char* nxt, unsigned voff) {
  if constexpr (TT + D < 8) issue(s, cur + (TT + D) * 4096, voff);
  else issue(s, nxt + (TT + D - 8) * 4096, voff);
}

template <int TT> __device__ __forceinline__ void kstep(Step (&ring)[D], const u32x4* tab, const unsigned (&qp)[16], int c,
                                                        float& A, float& Bs, const char* cur, const char* nxt, unsigned voff) {
  Step& s = ring[TT % D];
  float a0 = 0.f, a1 = 0.f, b0 = 0.f, b1 = 0.f;
#pragma unroll
  for (int j = 0; j < 4; ++j) {
    const u32x4 cs = tab[(TT * 4 + j) * 64];
    const float w0 = dot2(cs[0], qp[4 * j + 0]), w1 = dot2(cs[1], qp[4 * j + 1]);
    const float w2 = dot2(cs[2], qp[4 * j + 2]), w3 = dot2(cs[3], qp[4 * j + 3]);
    a0 = fmaf((float)s.v[j][0], w0, a0); a1 = fmaf((float)s.v[j][1], w1, a1);
    a0 = fmaf((float)s.v[j][2], w2, a0); a1 = fmaf((float)s.v[j][3], w3, a1);
    b0 += w0; b1 += w1; b0 += w2; b1 += w3;
  }
  refill<TT>(s, cur, nxt, voff);
  const float a = sum8(a0 + a1), b = sum8(b0 + b1);
  A = (c == TT) ? a : A;
  Bs = (c == TT) ? b : Bs;
}

template <int TT, bool LAST> __device__ __forceinline__ void vstep(Step (&ring)[D], float P, float (&acc)[16],
                                                                   const char* cur, const char* nxt, unsigned voff) {
  Step& s = ring[TT % D];
  const float p = bcast8<TT>(P);
#pragma unroll
  for (int j = 0; j < 4; ++j)
#pragma unroll
    for (int i = 0; i < 4; ++i) acc[4 * j + i] = fmaf((float)s.v[j][i], p, acc[4 * j + i]);
  if constexpr (!LAST || TT + D < 8) refill<TT>(s, cur, nxt, voff);
}

__global__ void __launch_bounds__(NTHR) attn_split(const unsigned short* __restrict__ q, const int* __restrict__ qk,
                                                   const unsigned short* __restrict__ ksc, const unsigned short* __restrict__ kzp,
                                                   const int* __restrict__ qv, const unsigned short* __restrict__ vsc,
                                                   const unsigned short* __restrict__ vzp, const unsigned short* __restrict__ qcos,
                                                   const unsigned short* __restrict__ qsin, const unsigned short* __restrict__ kcos,
                                                   const unsigned short* __restrict__ ksin, float* __restrict__ part) {
  __shared__ __attribute__((aligned(16))) u32x4 table[CHUNK * DH / 4];
  const int tid = threadIdx.x, lane = tid & 63, wave = __builtin_amdgcn_readfirstlane(tid >> 6);
  const int r = lane >> 3, c = lane & 7;
  const int bid = blockIdx.x, xcd = bid & 7, idx = bid >> 3;
  const int split = xcd * 4 + (idx & 3), head = (idx >> 2) * WAVES + wave;
  const int key0 = split * CHUNK;
  const size_t row0 = (size_t)head * SKV + key0;
  const char* kb = reinterpret_cast<const char*>(qk + row0 * DH);
  const char* vb = reinterpret_cast<const char*>(qv + row0 * DH);
  const unsigned voff = r * 512 + c * 16;

  u32x4 cc[8], ss[8];
#pragma unroll
  for (int i = 0; i < 8; ++i) {
    const int item = i * NTHR + tid, k = item >> 4, m = item & 15;
    cc[i] = *reinterpret_cast<const u32x4*>(kcos + (size_t)(key0 + k) * DH + 8 * m);
    ss[i] = *reinterpret_cast<const u32x4*>(ksin + (size_t)(key0 + k) * DH + ((8 * m + 64) & 127));
  }
  unsigned short qraw[16], qc[16], qs[16];
#pragma unroll
  for (int j = 0; j < 4; ++j)
#pragma unroll
    for (int i = 0; i < 4; ++i) {
      const int d = 32 * j + 4 * c + i;
      qraw[4 * j + i] = q[head * DH + d]; qc[4 * j + i] = qcos[d]; qs[4 * j + i] = qsin[d];
    }
  float ks[4], kz[4], vs[4], vz[4];
#pragma unroll
  for (int u = 0; u < 4; ++u) {
    const size_t e = row0 + 64 * u + 8 * c + r;
    ks[u] = bf2f(ksc[e]); kz[u] = bf2f(kzp[e]); vs[u] = bf2f(vsc[e]); vz[u] = bf2f(vzp[e]);
  }
  Step ring[D];
#pragma unroll
  for (int t = 0; t < D; ++t) issue(ring[t], kb + t * 4096, voff);

#pragma unroll
  for (int i = 0; i < 8; ++i) {
    const int item = i * NTHR + tid, k = item >> 4, m = item & 15;
    u32x4 lo, hi;
    lo[0] = __builtin_amdgcn_perm(ss[i][0], cc[i][0], 0x05040100u); lo[1] = __builtin_amdgcn_perm(ss[i][0], cc[i][0], 0x07060302u);
    lo[2] = __builtin_amdgcn_perm(ss[i][1], cc[i][1], 0x05040100u); lo[3] = __builtin_amdgcn_perm(ss[i][1], cc[i][1], 0x07060302u);
    hi[0] = __builtin_amdgcn_perm(ss[i][2], cc[i][2], 0x05040100u); hi[1] = __builtin_amdgcn_perm(ss[i][2], cc[i][2], 0x07060302u);
    hi[2] = __builtin_amdgcn_perm(ss[i][3], cc[i][3], 0x05040100u); hi[3] = __builtin_amdgcn_perm(ss[i][3], cc[i][3], 0x07060302u);
    const int slot = ((k >> 3) * 4 + (m >> 2)) * 64 + (k & 7) * 8 + ((2 * m) & 7);
    table[slot] = lo; table[slot + 1] = hi;
  }
  unsigned short qr[16];
#pragma unroll
  for (int e = 0; e < 16; ++e) {
    const float rot = e < 8 ? -bf2f(qraw[e + 8]) : bf2f(qraw[e - 8]);
    qr[e] = f2bf(rbf(bf2f(qraw[e]) * bf2f(qc[e])) + rbf(rot * bf2f(qs[e])));
  }
  unsigned qp[16];
#pragma unroll
  for (int e = 0; e < 16; ++e) {
    const unsigned other = e < 8 ? (unsigned)qr[e + 8] : ((unsigned)qr[e - 8] ^ 0x8000u);
    qp[e] = (unsigned)qr[e] | (other << 16);
  }
  __syncthreads();

  float A0 = 0.f, A1 = 0.f, A2 = 0.f, A3 = 0.f, B0 = 0.f, B1 = 0.f, B2 = 0.f, B3 = 0.f;
#pragma unroll 1
  for (int u = 0; u < 4; ++u) {
    const char* cur = kb + u * 32768;
    const char* nxt = u == 3 ? vb : cur + 32768;
    const u32x4* tab = table + u * 2048 + lane;
    float A = 0.f, Bs = 0.f;
    kstep<0>(ring, tab, qp, c, A, Bs, cur, nxt, voff); kstep<1>(ring, tab, qp, c, A, Bs, cur, nxt, voff);
    kstep<2>(ring, tab, qp, c, A, Bs, cur, nxt, voff); kstep<3>(ring, tab, qp, c, A, Bs, cur, nxt, voff);
    kstep<4>(ring, tab, qp, c, A, Bs, cur, nxt, voff); kstep<5>(ring, tab, qp, c, A, Bs, cur, nxt, voff);
    kstep<6>(ring, tab, qp, c, A, Bs, cur, nxt, voff); kstep<7>(ring, tab, qp, c, A, Bs, cur, nxt, voff);
    A0 = A1; A1 = A2; A2 = A3; A3 = A; B0 = B1; B1 = B2; B2 = B3; B3 = Bs;
  }

  constexpr float C2 = 0.088388347648318440f * 1.4426950408889634f;
  const float s0 = C2 * ks[0] * (A0 - kz[0] * B0), s1 = C2 * ks[1] * (A1 - kz[1] * B1);
  const float s2 = C2 * ks[2] * (A2 - kz[2] * B2), s3 = C2 * ks[3] * (A3 - kz[3] * B3);
  const float m = max_groups(max8(fmaxf(fmaxf(s0, s1), fmaxf(s2, s3))));
  const float p0 = __builtin_amdgcn_exp2f(s0 - m), p1 = __builtin_amdgcn_exp2f(s1 - m);
  const float p2 = __builtin_amdgcn_exp2f(s2 - m), p3 = __builtin_amdgcn_exp2f(s3 - m);
  float P0 = p0 * vs[0], P1 = p1 * vs[1], P2 = p2 * vs[2], P3 = p3 * vs[3];
  const float l = sum_groups(sum8((p0 + p1) + (p2 + p3)));
  const float corr = sum_groups(sum8((P0 * vz[0] + P1 * vz[1]) + (P2 * vz[2] + P3 * vz[3])));

  float acc[16];
#pragma unroll
  for (int e = 0; e < 16; ++e) acc[e] = 0.f;
#pragma unroll 1
  for (int u = 0; u < 3; ++u) {
    const char* cur = vb + u * 32768;
    const char* nxt = cur + 32768;
    vstep<0, false>(ring, P0, acc, cur, nxt, voff); vstep<1, false>(ring, P0, acc, cur, nxt, voff);
    vstep<2, false>(ring, P0, acc, cur, nxt, voff); vstep<3, false>(ring, P0, acc, cur, nxt, voff);
    vstep<4, false>(ring, P0, acc, cur, nxt, voff); vstep<5, false>(ring, P0, acc, cur, nxt, voff);
    vstep<6, false>(ring, P0, acc, cur, nxt, voff); vstep<7, false>(ring, P0, acc, cur, nxt, voff);
    P0 = P1; P1 = P2; P2 = P3;
  }
  {
    const char* cur = vb + 3 * 32768;
    vstep<0, true>(ring, P0, acc, cur, cur, voff); vstep<1, true>(ring, P0, acc, cur, cur, voff);
    vstep<2, true>(ring, P0, acc, cur, cur, voff); vstep<3, true>(ring, P0, acc, cur, cur, voff);
    vstep<4, true>(ring, P0, acc, cur, cur, voff); vstep<5, true>(ring, P0, acc, cur, cur, voff);
    vstep<6, true>(ring, P0, acc, cur, cur, voff); vstep<7, true>(ring, P0, acc, cur, cur, voff);
  }
#pragma unroll
  for (int e = 0; e < 16; ++e) acc[e] = sum_groups(acc[e]) - corr;
  float* rec = part + ((size_t)head * NSPLIT + split) * PREC;
  if (r < 4) {
    const f32x4 o = r == 0 ? f32x4{acc[0], acc[1], acc[2], acc[3]} : r == 1 ? f32x4{acc[4], acc[5], acc[6], acc[7]}
                  : r == 2 ? f32x4{acc[8], acc[9], acc[10], acc[11]} : f32x4{acc[12], acc[13], acc[14], acc[15]};
    *reinterpret_cast<f32x4*>(rec + 32 * r + 4 * c) = o;
  } else if (lane == 32) {
    *reinterpret_cast<f32x2*>(rec + 128) = f32x2{m, l};
  }
}

__global__ void __launch_bounds__(128) attn_combine(const float* __restrict__ part, unsigned short* __restrict__ out) {
  const int head = blockIdx.x, d = threadIdx.x;
  const float* rec = part + (size_t)head * NSPLIT * PREC;
  float a[NSPLIT];
  f32x2 ml[NSPLIT];
#pragma unroll
  for (int s = 0; s < NSPLIT; ++s) {
    a[s] = rec[s * PREC + d];
    ml[s] = *reinterpret_cast<const f32x2*>(rec + s * PREC + 128);
  }
  float M = ml[0][0];
#pragma unroll
  for (int s = 1; s < NSPLIT; ++s) M = fmaxf(M, ml[s][0]);
  float L = 0.f, o = 0.f;
#pragma unroll
  for (int s = 0; s < NSPLIT; ++s) {
    const float w = __builtin_amdgcn_exp2f(ml[s][0] - M);
    L = fmaf(w, ml[s][1], L); o = fmaf(w, a[s], o);
  }
  out[head * DH + d] = f2bf(o / L);
}

}

extern "C" void kernel_launch(void* const* d_in, const int* in_sizes, int n_in, void* d_out, int out_size, void* d_ws,
                              size_t ws_size, hipStream_t stream) {
  using us = const unsigned short*;
  float* part = reinterpret_cast<float*>(d_ws);
  attn_split<<<NWG, NTHR, 0, stream>>>((us)d_in[0], (const int*)d_in[1], (us)d_in[2], (us)d_in[3], (const int*)d_in[4],
                                       (us)d_in[5], (us)d_in[6], (us)d_in[7], (us)d_in[8], (us)d_in[9], (us)d_in[10], part);
  attn_combine<<<NHEAD, 128, 0, stream>>>(part, (unsigned short*)d_out);
}
```

```cpp
#include <hip/hip_runtime.h>
#include <hip/hip_bf16.h>
#include <cstdint>

namespace {

constexpr int NB = 8, NH = 8, SKV = 8192, DH = 128;
constexpr int NHEAD = NB * NH;
constexpr int CHUNK = 256;
constexpr int NSPLIT = SKV / CHUNK;
constexpr int WAVES = 8;
constexpr int NTHR = WAVES * 64;
constexpr int NWG = NSPLIT * NHEAD / WAVES;
#define DEPTH 4
constexpr int D = DEPTH;
constexpr int PREC = 132;
constexpr int KAUX = 2, VAUX = 2;

using u32x4 = __attribute__((ext_vector_type(4))) unsigned;
using i32x4 = __attribute__((ext_vector_type(4))) int;
using f32x2 = __attribute__((ext_vector_type(2))) float;
using f32x4 = __attribute__((ext_vector_type(4))) float;
using bf16x2 = __attribute__((ext_vector_type(2))) __bf16;

__device__ __forceinline__ float bf2f(unsigned short x) { return __uint_as_float(((unsigned)x) << 16); }
__device__ __forceinline__ unsigned short f2bf(float x) { return __builtin_bit_cast(unsigned short, (__bf16)x); }
__device__ __forceinline__ float rbf(float x) { return bf2f(f2bf(x)); }
__device__ __forceinline__ float dot2(unsigned a, unsigned b) {
  return __builtin_amdgcn_fdot2_f32_bf16(__builtin_bit_cast(bf16x2, a), __builtin_bit_cast(bf16x2, b), 0.0f, false);
}
template <int CTRL> __device__ __forceinline__ float dpp(float x) {
  return __builtin_bit_cast(float, __builtin_amdgcn_mov_dpp(__builtin_bit_cast(int, x), CTRL, 0xf, 0xf, true));
}
constexpr int XOR1 = 0xB1, XOR2 = 0x4E, HMIR = 0x141, ROR8 = 0x128;
__device__ __forceinline__ float sum8(float x) {
  x += dpp<XOR1>(x); x += dpp<XOR2>(x); x += dpp<HMIR>(x); return x;
}
__device__ __forceinline__ float max8(float x) {
  x = fmaxf(x, dpp<XOR1>(x)); x = fmaxf(x, dpp<XOR2>(x)); x = fmaxf(x, dpp<HMIR>(x)); return x;
}
__device__ __forceinline__ float sum_groups(float x) {
  x += dpp<ROR8>(x);
  auto s = __builtin_amdgcn_permlane16_swap(__float_as_uint(x), __float_as_uint(x), false, false);
  x = __uint_as_float(s[0]) + __uint_as_float(s[1]);
  auto t = __builtin_amdgcn_permlane32_swap(__float_as_uint(x), __float_as_uint(x), false, false);
  return __uint_as_float(t[0]) + __uint_as_float(t[1]);
}
__device__ __forceinline__ float max_groups(float x) {
  x = fmaxf(x, dpp<ROR8>(x));
  auto s = __builtin_amdgcn_permlane16_swap(__float_as_uint(x), __float_as_uint(x), false, false);
  x = fmaxf(__uint_as_float(s[0]), __uint_as_float(s[1]));
  auto t = __builtin_amdgcn_permlane32_swap(__float_as_uint(x), __float_as_uint(x), false, false);
  return fmaxf(__uint_as_float(t[0]), __uint_as_float(t[1]));
}
template <int C> __device__ __forceinline__ float bcast8(float x) {
  constexpr int q = C & 3, QP = q | (q << 2) | (q << 4) | (q << 6);
  const int a = __builtin_amdgcn_mov_dpp(__builtin_bit_cast(int, x), QP, 0xf, 0xf, true);
  return __builtin_bit_cast(float, __builtin_amdgcn_update_dpp(a, a, HMIR, 0xf, C < 4 ? 0xa : 0x5, false));
}

struct Step { i32x4 v[4]; };

template <int AUX> __device__ __forceinline__ void issue(Step& s, const char* p, unsigned voff) {
  auto rs = __builtin_amdgcn_make_buffer_rsrc((void*)p, 0, 4096, 0x00020000);
#pragma unroll
  for (int j = 0; j < 4; ++j) s.v[j] = __builtin_bit_cast(i32x4, __builtin_amdgcn_raw_buffer_load_b128(rs, voff + j * 128, 0, AUX));
}

template <int TT, int AUX> __device__ __forceinline__ void refill(Step& s, const char* cur, const char* nxt, unsigned voff) {
  __builtin_amdgcn_sched_barrier(0);
  if constexpr (TT + D < 8) issue<AUX>(s, cur + (TT + D) * 4096, voff);
  else issue<AUX>(s, nxt + (TT + D - 8) * 4096, voff);
  __builtin_amdgcn_sched_barrier(0);
}

template <int TT> __device__ __forceinline__ void kstep(Step (&ring)[D], const u32x4* tab, const unsigned (&qp)[16], int c,
                                                        float& A, float& Bs, const char* cur, const char* nxt, unsigned voff) {
  Step& s = ring[TT % D];
  float a0 = 0.f, a1 = 0.f, b0 = 0.f, b1 = 0.f;
#pragma unroll
  for (int j = 0; j < 4; ++j) {
    const u32x4 cs = tab[(TT * 4 + j) * 64];
    const float w0 = dot2(cs[0], qp[4 * j + 0]), w1 = dot2(cs[1], qp[4 * j + 1]);
    const float w2 = dot2(cs[2], qp[4 * j + 2]), w3 = dot2(cs[3], qp[4 * j + 3]);
    a0 = fmaf((float)s.v[j][0], w0, a0); a1 = fmaf((float)s.v[j][1], w1, a1);
    a0 = fmaf((float)s.v[j][2], w2, a0); a1 = fmaf((float)s.v[j][3], w3, a1);
    b0 += w0; b1 += w1; b0 += w2; b1 += w3;
  }
  refill<TT, KAUX>(s, cur, nxt, voff);
  const float a = sum8(a0 + a1), b = sum8(b0 + b1);
  A = (c == TT) ? a : A;
  Bs = (c == TT) ? b : Bs;
}

template <int TT, bool LAST> __device__ __forceinline__ void vstep(Step (&ring)[D], float P, float (&acc)[16],
                                                                   const char* cur, const char* nxt, unsigned voff) {
  Step& s = ring[TT % D];
  const float p = bcast8<TT>(P);
#pragma unroll
  for (int j = 0; j < 4; ++j)
#pragma unroll
    for (int i = 0; i < 4; ++i) acc[4 * j + i] = fmaf((float)s.v[j][i], p, acc[4 * j + i]);
  if constexpr (!LAST || TT + D < 8) refill<TT, VAUX>(s, cur, nxt, voff);
  else __builtin_amdgcn_sched_barrier(0);
}

__global__ void __launch_bounds__(NTHR) attn_split(const unsigned short* __restrict__ q, const int* __restrict__ qk,
                                                   const unsigned short* __restrict__ ksc, const unsigned short* __restrict__ kzp,
                                                   const int* __restrict__ qv, const unsigned short* __restrict__ vsc,
                                                   const unsigned short* __restrict__ vzp, const unsigned short* __restrict__ qcos,
                                                   const unsigned short* __restrict__ qsin, const unsigned short* __restrict__ kcos,
                                                   const unsigned short* __restrict__ ksin, float* __restrict__ part) {
  __shared__ __attribute__((aligned(16))) u32x4 table[CHUNK * DH / 4];
  const int tid = threadIdx.x, lane = tid & 63, wave = __builtin_amdgcn_readfirstlane(tid >> 6);
  const int r = lane >> 3, c = lane & 7;
  const int bid = blockIdx.x, xcd = bid & 7, idx = bid >> 3;
  const int split = xcd * 4 + (idx & 3), head = (idx >> 2) * WAVES + wave;
  const int key0 = split * CHUNK;
  const size_t row0 = (size_t)head * SKV + key0;
  const char* kb = reinterpret_cast<const char*>(qk + row0 * DH);
  const char* vb = reinterpret_cast<const char*>(qv + row0 * DH);
  const unsigned voff = r * 512 + c * 16;

  u32x4 cc[8], ss[8];
#pragma unroll
  for (int i = 0; i < 8; ++i) {
    const int item = i * NTHR + tid, k = item >> 4, m = item & 15;
    cc[i] = *reinterpret_cast<const u32x4*>(kcos + (size_t)(key0 + k) * DH + 8 * m);
    ss[i] = *reinterpret_cast<const u32x4*>(ksin + (size_t)(key0 + k) * DH + ((8 * m + 64) & 127));
  }
  unsigned short qraw[16], qc[16], qs[16];
#pragma unroll
  for (int j = 0; j < 4; ++j)
#pragma unroll
    for (int i = 0; i < 4; ++i) {
      const int d = 32 * j + 4 * c + i;
      qraw[4 * j + i] = q[head * DH + d]; qc[4 * j + i] = qcos[d]; qs[4 * j + i] = qsin[d];
    }
  float ks[4], kz[4], vs[4], vz[4];
#pragma unroll
  for (int u = 0; u < 4; ++u) {
    const size_t e = row0 + 64 * u + 8 * c + r;
    ks[u] = bf2f(ksc[e]); kz[u] = bf2f(kzp[e]); vs[u] = bf2f(vsc[e]); vz[u] = bf2f(vzp[e]);
  }
  Step ring[D];
  __builtin_amdgcn_sched_barrier(0);
#pragma unroll
  for (int t = 0; t < D; ++t) issue<KAUX>(ring[t], kb + t * 4096, voff);
  __builtin_amdgcn_sched_barrier(0);

#pragma unroll
  for (int i = 0; i < 8; ++i) {
    const int item = i * NTHR + tid, k = item >> 4, m = item & 15;
    u32x4 lo, hi;
    lo[0] = __builtin_amdgcn_perm(ss[i][0], cc[i][0], 0x05040100u); lo[1] = __builtin_amdgcn_perm(ss[i][0], cc[i][0], 0x07060302u);
    lo[2] = __builtin_amdgcn_perm(ss[i][1], cc[i][1], 0x05040100u); lo[3] = __builtin_amdgcn_perm(ss[i][1], cc[i][1], 0x07060302u);
    hi[0] = __builtin_amdgcn_perm(ss[i][2], cc[i][2], 0x05040100u); hi[1] = __builtin_amdgcn_perm(ss[i][2], cc[i][2], 0x07060302u);
    hi[2] = __builtin_amdgcn_perm(ss[i][3], cc[i][3], 0x05040100u); hi[3] = __builtin_amdgcn_perm(ss[i][3], cc[i][3], 0x07060302u);
    const int slot = ((k >> 3) * 4 + (m >> 2)) * 64 + (k & 7) * 8 + ((2 * m) & 7);
    table[slot] = lo; table[slot + 1] = hi;
  }
  unsigned short qr[16];
#pragma unroll
  for (int e = 0; e < 16; ++e) {
    const float rot = e < 8 ? -bf2f(qraw[e + 8]) : bf2f(qraw[e - 8]);
    qr[e] = f2bf(rbf(bf2f(qraw[e]) * bf2f(qc[e])) + rbf(rot * bf2f(qs[e])));
  }
  unsigned qp[16];
#pragma unroll
  for (int e = 0; e < 16; ++e) {
    const unsigned other = e < 8 ? (unsigned)qr[e + 8] : ((unsigned)qr[e - 8] ^ 0x8000u);
    qp[e] = (unsigned)qr[e] | (other << 16);
  }
  __syncthreads();

  float A0 = 0.f, A1 = 0.f, A2 = 0.f, A3 = 0.f, B0 = 0.f, B1 = 0.f, B2 = 0.f, B3 = 0.f;
#pragma unroll 1
  for (int u = 0; u < 4; ++u) {
    const char* cur = kb + u * 32768;
    const char* nxt = u == 3 ? vb : cur + 32768;
    const u32x4* tab = table + u * 2048 + lane;
    float A = 0.f, Bs = 0.f;
    kstep<0>(ring, tab, qp, c, A, Bs, cur, nxt, voff); kstep<1>(ring, tab, qp, c, A, Bs, cur, nxt, voff);
    kstep<2>(ring, tab, qp, c, A, Bs, cur, nxt, voff); kstep<3>(ring, tab, qp, c, A, Bs, cur, nxt, voff);
    kstep<4>(ring, tab, qp, c, A, Bs, cur, nxt, voff); kstep<5>(ring, tab, qp, c, A, Bs, cur, nxt, voff);
    kstep<6>(ring, tab, qp, c, A, Bs, cur, nxt, voff); kstep<7>(ring, tab, qp, c, A, Bs, cur, nxt, voff);
    A0 = A1; A1 = A2; A2 = A3; A3 = A; B0 = B1; B1 = B2; B2 = B3; B3 = Bs;
  }

  constexpr float C2 = 0.088388347648318440f * 1.4426950408889634f;
  const float s0 = C2 * ks[0] * (A0 - kz[0] * B0), s1 = C2 * ks[1] * (A1 - kz[1] * B1);
  const float s2 = C2 * ks[2] * (A2 - kz[2] * B2), s3 = C2 * ks[3] * (A3 - kz[3] * B3);
  const float m = max_groups(max8(fmaxf(fmaxf(s0, s1), fmaxf(s2, s3))));
  const float p0 = __builtin_amdgcn_exp2f(s0 - m), p1 = __builtin_amdgcn_exp2f(s1 - m);
  const float p2 = __builtin_amdgcn_exp2f(s2 - m), p3 = __builtin_amdgcn_exp2f(s3 - m);
  float P0 = p0 * vs[0], P1 = p1 * vs[1], P2 = p2 * vs[2], P3 = p3 * vs[3];
  const float l = sum_groups(sum8((p0 + p1) + (p2 + p3)));
  const float corr = sum_groups(sum8((P0 * vz[0] + P1 * vz[1]) + (P2 * vz[2] + P3 * vz[3])));

  float acc[16];
#pragma unroll
  for (int e = 0; e < 16; ++e) acc[e] = 0.f;
#pragma unroll 1
  for (int u = 0; u < 3; ++u) {
    const char* cur = vb + u * 32768;
    const char* nxt = cur + 32768;
    vstep<0, false>(ring, P0, acc, cur, nxt, voff); vstep<1, false>(ring, P0, acc, cur, nxt, voff);
    vstep<2, false>(ring, P0, acc, cur, nxt, voff); vstep<3, false>(ring, P0, acc, cur, nxt, voff);
    vstep<4, false>(ring, P0, acc, cur, nxt, voff); vstep<5, false>(ring, P0, acc, cur, nxt, voff);
    vstep<6, false>(ring, P0, acc, cur, nxt, voff); vstep<7, false>(ring, P0, acc, cur, nxt, voff);
    P0 = P1; P1 = P2; P2 = P3;
  }
  {
    const char* cur = vb + 3 * 32768;
    vstep<0, true>(ring, P0, acc, cur, cur, voff); vstep<1, true>(ring, P0, acc, cur, cur, voff);
    vstep<2, true>(ring, P0, acc, cur, cur, voff); vstep<3, true>(ring, P0, acc, cur, cur, voff);
    vstep<4, true>(ring, P0, acc, cur, cur, voff); vstep<5, true>(ring, P0, acc, cur, cur, voff);
    vstep<6, true>(ring, P0, acc, cur, cur, voff); vstep<7, true>(ring, P0, acc, cur, cur, voff);
  }
#pragma unroll
  for (int e = 0; e < 16; ++e) acc[e] = sum_groups(acc[e]) - corr;
  float* rec = part + ((size_t)head * NSPLIT + split) * PREC;
  if (r < 4) {
    const f32x4 o = r == 0 ? f32x4{acc[0], acc[1], acc[2], acc[3]} : r == 1 ? f32x4{acc[4], acc[5], acc[6], acc[7]}
                  : r == 2 ? f32x4{acc[8], acc[9], acc[10], acc[11]} : f32x4{acc[12], acc[13], acc[14], acc[15]};
    *reinterpret_cast<f32x4*>(rec + 32 * r + 4 * c) = o;
  } else if (lane == 32) {
    *reinterpret_cast<f32x2*>(rec + 128) = f32x2{m, l};
  }
}

__global__ void __launch_bounds__(128) attn_combine(const float* __restrict__ part, unsigned short* __restrict__ out) {
  const int head = blockIdx.x, d = threadIdx.x;
  const float* rec = part + (size_t)head * NSPLIT * PREC;
  float a[NSPLIT];
  f32x2 ml[NSPLIT];
#pragma unroll
  for (int s = 0; s < NSPLIT; ++s) {
    a[s] = rec[s * PREC + d];
    ml[s] = *reinterpret_cast<const f32x2*>(rec + s * PREC + 128);
  }
  float M = ml[0][0];
#pragma unroll
  for (int s = 1; s < NSPLIT; ++s) M = fmaxf(M, ml[s][0]);
  float L = 0.f, o = 0.f;
#pragma unroll
  for (int s = 0; s < NSPLIT; ++s) {
    const float w = __builtin_amdgcn_exp2f(ml[s][0] - M);
    L = fmaf(w, ml[s][1], L); o = fmaf(w, a[s], o);
  }
  out[head * DH + d] = f2bf(o / L);
}

}

extern "C" void kernel_launch(void* const* d_in, const int* in_sizes, int n_in, void* d_out, int out_size, void* d_ws,
                              size_t ws_size, hipStream_t stream) {
  using us = const unsigned short*;
  float* part = reinterpret_cast<float*>(d_ws);
  attn_split<<<NWG, NTHR, 0, stream>>>((us)d_in[0], (const int*)d_in[1], (us)d_in[2], (us)d_in[3], (const int*)d_in[4],
                                       (us)d_in[5], (us)d_in[6], (us)d_in[7], (us)d_in[8], (us)d_in[9], (us)d_in[10], part);
  attn_combine<<<NHEAD, 128, 0, stream>>>(part, (unsigned short*)d_out);
}
```

```cpp
#include <hip/hip_runtime.h>
#include <hip/hip_bf16.h>
#include <cstdint>

namespace {

constexpr int NB = 8, NH = 8, SKV = 8192, DH = 128;
constexpr int NHEAD = NB * NH;
constexpr int CHUNK = 256;
constexpr int NSPLIT = SKV / CHUNK;
constexpr int NC = 8;
constexpr int NTHR = (NC + 2) * 64;
constexpr int NWG = NSPLIT * NH;
#define PSLEEP 1
#define PACE 6800
#define REANCHOR 1
#define OUTPF 0
#define OUTAUX 0
#define GDELAY 16
#define SENDAUX SC1
#define QDELAY 0
#define TDELAY 1
#define TSTEP 2
#define GD 6
#define NMIN 10
#define NMAX 26
#define NMID 20
#define NSTART 18
#define GAIN 64
#define MXW 8
#define MXC 12
#define MXD 8
#define EARLY 0
#define GDELAY0 16
#define EXST 0
#define EXAUX SC1
#define MLST 0
#define ARST 0
#define XN 16
#define EXS 1
#define GSLEEP 0
#define SPR 0
#define EP0 24
#define EPD 8
#define EPS 16
#define ANCHOR 4
constexpr int ROUNDS = NB * 2 * 4;

constexpr int L_RING = 0, RING_BYTES = 131072;
constexpr int L_SC = RING_BYTES;
constexpr int L_QP = L_SC + 16384;
constexpr int L_MX = L_QP + 4096;
constexpr int L_SM = L_MX + 256;
constexpr int L_FLAG = L_SM + 256;
constexpr int L_LAND = L_FLAG + 256;
constexpr int L_VS = L_LAND + 256;
constexpr int LDS_BYTES = L_VS + 256;

#define SPIN_DECL
#define SPIN_OK
#define LAS __attribute__((address_space(3)))
using u32x2 = __attribute__((ext_vector_type(2))) unsigned;
using u32x4 = __attribute__((ext_vector_type(4))) unsigned;
using i32x4 = __attribute__((ext_vector_type(4))) int;
using f32x2 = __attribute__((ext_vector_type(2))) float;
using f32x4 = __attribute__((ext_vector_type(4))) float;
using bf16x2 = __attribute__((ext_vector_type(2))) __bf16;
using us = unsigned short;

__device__ __forceinline__ float bf2f(us x) { return __uint_as_float(((unsigned)x) << 16); }
__device__ __forceinline__ us f2bf(float x) { return __builtin_bit_cast(us, (__bf16)x); }
__device__ __forceinline__ float rbf(float x) { return bf2f(f2bf(x)); }
__device__ __forceinline__ float dot2(unsigned a, unsigned b) {
  return __builtin_amdgcn_fdot2_f32_bf16(__builtin_bit_cast(bf16x2, a), __builtin_bit_cast(bf16x2, b), 0.0f, false);
}
template <int CTRL> __device__ __forceinline__ float dpp(float x) {
  return __builtin_bit_cast(float, __builtin_amdgcn_mov_dpp(__builtin_bit_cast(int, x), CTRL, 0xf, 0xf, true));
}
constexpr int XOR1 = 0xB1, XOR2 = 0x4E, HMIR = 0x141, ROR8 = 0x128;
__device__ __forceinline__ float swap16_add(float x, float y) {
  auto s = __builtin_amdgcn_permlane16_swap(__float_as_uint(x), __float_as_uint(y), false, false);
  return __uint_as_float(s[0]) + __uint_as_float(s[1]);
}
__device__ __forceinline__ float swap32_add(float x, float y) {
  auto s = __builtin_amdgcn_permlane32_swap(__float_as_uint(x), __float_as_uint(y), false, false);
  return __uint_as_float(s[0]) + __uint_as_float(s[1]);
}
__device__ __forceinline__ float sum8(float x) { x += dpp<XOR1>(x); x += dpp<XOR2>(x); x += dpp<HMIR>(x); return x; }
__device__ __forceinline__ float max8(float x) { x = fmaxf(x, dpp<XOR1>(x)); x = fmaxf(x, dpp<XOR2>(x)); return fmaxf(x, dpp<HMIR>(x)); }
__device__ __forceinline__ float sum32(float x) {
  x += dpp<XOR1>(x); x += dpp<XOR2>(x); x += dpp<ROR8>(x);
  x = swap16_add(x, x); return swap32_add(x, x);
}
__device__ __forceinline__ float max32(float x) {
  x = fmaxf(x, dpp<XOR1>(x)); x = fmaxf(x, dpp<XOR2>(x)); x = fmaxf(x, dpp<ROR8>(x));
  auto s = __builtin_amdgcn_permlane16_swap(__float_as_uint(x), __float_as_uint(x), false, false);
  x = fmaxf(__uint_as_float(s[0]), __uint_as_float(s[1]));
  auto t = __builtin_amdgcn_permlane32_swap(__float_as_uint(x), __float_as_uint(x), false, false);
  return fmaxf(__uint_as_float(t[0]), __uint_as_float(t[1]));
}
template <int N> __device__ __forceinline__ void wait_vm() { asm volatile("s_waitcnt vmcnt(%0)" ::"n"(N) : "memory"); }
__device__ __forceinline__ void lds_put(unsigned addr, unsigned val) { asm volatile("ds_write_b32 %0, %1" ::"v"(addr), "v"(val) : "memory"); }
__device__ __forceinline__ unsigned lds_get(unsigned addr) {
  unsigned v;
  asm volatile("ds_read_b32 %0, %1\n\ts_waitcnt lgkmcnt(0)" : "=v"(v) : "v"(addr) : "memory");
  return v;
}
__device__ __forceinline__ void wait_flag(unsigned addr, int need) {
  SPIN_DECL;
  while (__builtin_amdgcn_readfirstlane((int)lds_get(addr)) < need SPIN_OK) __builtin_amdgcn_s_sleep(PSLEEP);
}

#define RSTAMP(x)
#define PUT(i, x)
#define CSTAMP(x)
#define CPUT(i, x)
struct Args {
  const int* qk; const int* qv; const us* ksc; const us* kzp; const us* vsc; const us* vzp; const us* kcos; const us* ksin;
  const us* q; const us* qcos; const us* qsin; unsigned char* ws; us* out;
};

constexpr unsigned WS_EX = 0;
constexpr unsigned WS_ML = 1u << 18;
constexpr unsigned WS_IN = 1u << 20;
constexpr unsigned WS_BYTES = 2u << 20;
constexpr int SC1 = 16;
constexpr int SPIN_CAP = 1 << 17;
constexpr int NEPOCH = (ROUNDS * 8 - 16 - EPD - EP0 + EPS - 1) / EPS;
constexpr int E_POST = (36 * 8 + 1 - EP0 + EPS - 1) / EPS, E_ARM = E_POST + (63 + EPS) / EPS;
static_assert(L_LAND == L_FLAG + 256, "the loader clears both areas from one address");
static_assert(E_ARM + MLST + ARST < NEPOCH && NEPOCH <= 32, "epoch schedule");

#define MAPSPEC "76hg5fec4d3ba210 d>0 f>1 e>2"
constexpr unsigned map_mask(char ch) {
  constexpr char spec[] = MAPSPEC;
  unsigned m = 0;
  for (int p = 0; p < 16; ++p) if (spec[15 - p] == ch) m |= 1u << p;
  for (unsigned i = 16; i + 3 < sizeof(spec); i += 4) if (spec[i + 1] == ch) m ^= 1u << (spec[i + 3] - '0');
  return m;
}
template <int N> __device__ __forceinline__ unsigned map_bits(unsigned v, char first) {
  unsigned m = 0;
#pragma unroll
  for (int i = 0; i < N; ++i) m ^= ((v >> i) & 1) ? map_mask(first + i) : 0u;
  return m;
}

#define STR_(x) #x
#define STR(x) STR_(x)
#define GCHK(I, S)                                                                           \
  "v_xor_b32 v[96+4*" #I "], v[64+4*" #I "], v[80+4*" #I "]\n\t"                             \
  "v_xor_b32 v[97+4*" #I "], v[65+4*" #I "], v[81+4*" #I "]\n\t"                             \
  "v_xor_b32 v[98+4*" #I "], v[66+4*" #I "], v[82+4*" #I "]\n\t"                             \
  "v_xor_b32 v[99+4*" #I "], v[67+4*" #I "], v[83+4*" #I "]\n\t"                             \
  "v_bitop3_b32 %[t0], v[96+4*" #I "], v[97+4*" #I "], 1 bitop3:0x80\n\t"                    \
  "v_bitop3_b32 %[t0], v[98+4*" #I "], v[99+4*" #I "], %[t0] bitop3:0x80\n\t"                \
  "v_cmp_eq_u32 s[" #S ":" #S "+1], 0, %[t0]\n\t"
#define GLD(I, S, OFF) "s_mov_b64 exec, s[" #S ":" #S "+1]\n\tbuffer_load_dwordx4 v[64+4*" #I ":67+4*" #I "], %[ino], %[rs], 0 offen offset:" #OFF " sc1\n\t"
#define LDK0(I)                                                                              \
  "s_xor_b32 s52, s53, " #I "*0x1000\n\t"                                                    \
  "buffer_load_dwordx4 %[voff], s[40:43], s52 offen nt lds\n\t"                              \
  "buffer_load_dwordx4 %[voff], s[40:43], s52 offen offset:1024 nt lds\n\t"                  \
  "buffer_load_dwordx4 %[voff], s[40:43], s52 offen offset:2048 nt lds\n\t"                  \
  "buffer_load_dwordx4 %[voff], s[40:43], s52 offen offset:3072 nt lds\n\t"                  \
  "s_add_u32 m0, m0, 0x1000\n\t"
#define LDSC1(SO) "buffer_load_dwordx4 %[sco], s[60:63], " SO " offen nt lds\n\ts_add_u32 m0, m0, 0x400\n\ts_nop 0\n\t"
#define LDSC(P) "s_mov_b64 s[60:61], " P "\n\ts_and_b32 s61, s61, 0xffff\n\t" LDSC1("0") LDSC1("s50") LDSC1("s51") LDSC1("s52")
#define LDTB1(SO) "buffer_load_dwordx4 %[voff], s[60:63], " SO " offen lds\n\tbuffer_load_dwordx4 %[voff], s[60:63], " SO " offen offset:1024 lds\n\t"
#define LDTB(P) "s_mov_b64 s[60:61], " P "\n\ts_and_b32 s61, s61, 0xffff\n\t" LDTB1("%[tbo]") LDTB1("s50") LDTB1("s51") LDTB1("s52")
#define DRAIN(N) "v_max_i32 %[dv], 512-" #N "/4, %[dv]\n\ts_waitcnt vmcnt(" #N ")\n\tds_write_b32 %[flag], %[dv]\n\t"
#define SPIN_ASM

__global__ void __launch_bounds__(NTHR) attn_fused(Args a) {
  __shared__ __attribute__((aligned(16))) unsigned char smem[LDS_BYTES];
  LAS unsigned char* L = (LAS unsigned char*)smem;
  const unsigned lbase = (unsigned)(uintptr_t)L;
  const int tid = threadIdx.x, wave = __builtin_amdgcn_readfirstlane(tid >> 6);
  int lane = tid & 63;
  const int bid = blockIdx.x;
  const unsigned cub = map_bits<8>(bid, 'a');

  {
    const unsigned voff = lane * 16;
    asm volatile(
        "s_cmp_lg_u32 %[wave], 0\n\t"
        "s_cbranch_scc1 9f\n\t"
        "s_setprio 3\n\t"
        "s_mov_b64 s[40:41], %[kp]\n\ts_and_b32 s41, s41, 0xffff\n\ts_mov_b32 s42, 0x10000000\n\ts_mov_b32 s43, 0x20000\n\t"
        "s_mov_b32 m0, %[ring]\n\t"
        "s_lshl_b32 s53, %[cub], 12\n\t"
        "ds_write_b64 %[init], %[zero]\n\t"
        LDK0(0) LDK0(1) LDK0(2) LDK0(3)
        "s_waitcnt lgkmcnt(0)\n"
        "9:\n\t"
        "s_barrier"
        :
        : [voff] "v"(voff), [init] "v"(lbase + L_FLAG + lane * 8), [zero] "v"(0ull), [wave] "s"(wave), [cub] "s"(cub),
          [ring] "s"(lbase + L_RING), [kp] "s"(a.qk)
        : "memory", "scc", "s40", "s41", "s42", "s43", "s52", "s53");
    __builtin_amdgcn_sched_barrier(0);
    asm volatile("" : "+v"(lane));
    const unsigned sco = ((cub ^ map_bits<2>(lane >> 3, '3') ^ map_bits<1>(lane >> 5, '5')) & ~7u) * 16 + (lane & 7) * 16;
    const unsigned rt = lane < 32 ? lane >> 3 : lane & 3, rn = lane < 32 ? lane & 7 : (lane >> 2) & 7;
    const unsigned roff = (cub ^ map_bits<2>(rt, '3') ^ map_bits<3>(rn, '5')) << 12;
    const unsigned flag = lbase + L_LAND + lane * 4, dflag = lbase + L_FLAG + 64 + (lane & 7) * 4;
    const unsigned aflag = lbase + L_FLAG + 8, vflag = lbase + L_VS + lane * 4;
    unsigned vt, dv, av;
    asm volatile(
        "s_cmp_lg_u32 %[wave], 0\n\t"
        "s_cbranch_scc1 9f\n\t"
        "s_mov_b32 s62, 0x100000\n\ts_mov_b32 s63, 0x20000\n\t"
        "s_add_u32 m0, %[ring], %[lsc]\n\t"
        "s_mov_b32 s50, %[n1]\n\ts_mov_b32 s51, %[n2]\n\ts_mov_b32 s52, %[n1]+%[n2]\n\t"
        LDSC("%[ksc]") LDSC("%[kzp]") LDSC("%[vsc]") LDSC("%[vzp]")
        "s_mov_b32 s62, 0x200000\n\t"
        "s_add_u32 m0, %[ring], 126976\n\t"
        "s_xor_b32 s50, %[tbo], %[t1]\n\ts_xor_b32 s51, %[tbo], %[t2]\n\ts_xor_b32 s52, %[tbo], %[t1]+%[t2]\n\t"
        LDTB("%[kcos]") LDTB("%[ksin]")
        "s_mov_b64 s[40:41], %[kp]\n\ts_and_b32 s41, s41, 0xffff\n\ts_mov_b32 s42, 0x10000000\n\ts_mov_b32 s43, 0x20000\n\t"
        "s_mov_b64 s[58:59], %[vp]\n\ts_and_b32 s59, s59, 0xffff\n\t"
        "s_mov_b32 s46, 4\n\ts_mov_b32 s47, 0\n\ts_mov_b32 s49, 0\n\ts_mov_b32 s55, %[skew]\n\ts_mov_b32 s56, 0\n\ts_mov_b32 s57, 0\n\t"
        "s_mov_b32 s54, " STR(NSTART) "-" STR(NMIN) "\n\t"
        "v_readlane_b32 s53, %[roff], 0\n\t"
        "ds_read_b32 %[dv], %[dflag]\n\t"
        "ds_read_b32 %[av], %[aflag]\n\t"
        "s_memrealtime s[44:45]\n\t"
        "s_add_u32 m0, %[ring], 0x4000\n\t"
        "s_xor_b32 s52, s53, 0x4000\n\t"
        "s_getpc_b64 s[60:61]\n"
        "8:\n\t"
        "s_add_u32 s60, s60, 1f-8b\n\t"
        "s_addc_u32 s61, s61, 0\n\t"
        "s_waitcnt vmcnt(" STR(NSTART) ")\n\t"
        "s_branch 2f\n"
        "1:\n\t"
        ".set kvq_n, " STR(NMIN) "\n\t.rept " STR(NMAX) "-" STR(NMIN) "+1\n\ts_waitcnt vmcnt(kvq_n)\n\t.set kvq_n, kvq_n+1\n\t.endr\n"
        "2:\n\t"
        "buffer_load_dwordx4 %[voff], s[40:43], s52 offen nt lds\n\t"
        "buffer_load_dwordx4 %[voff], s[40:43], s52 offen offset:1024 nt lds\n\t"
        "buffer_load_dwordx4 %[voff], s[40:43], s52 offen offset:2048 nt lds\n\t"
        "buffer_load_dwordx4 %[voff], s[40:43], s52 offen offset:3072 nt lds\n\t"
        "v_mov_b32 %[vt], s47\n\t"
        "s_lshr_b32 s50, s55, 9\n\t"
        "s_waitcnt lgkmcnt(0)\n\t"
        "ds_write_b32 %[flag], %[vt]\n\t"
        "s_sub_u32 s48, s44, s50\n\t"
        "s_memrealtime s[44:45]\n\t"
        "s_sub_i32 s51, s48, s49\n\t"
        "s_mul_i32 s51, s51, s57\n\t"
        "s_ashr_i32 s51, s51, 8\n\t"
        "s_add_i32 s51, s51, s54\n\t"
        "s_max_i32 s51, s51, 0\n\t"
        "s_min_i32 s51, s51, " STR(NMAX) "-" STR(NMIN) "\n\t"
        "s_lshl2_add_u32 s62, s51, s60\n\t"
        "s_addc_u32 s63, s61, 0\n\t"
        "s_add_u32 s55, s55, " STR(PACE) "\n\t"
        "s_add_u32 s46, s46, 1\n\t"
        "s_add_u32 s51, s51, " STR(NMIN) "+3\n\t"
        "s_lshr_b32 s51, s51, 2\n\t"
        "s_sub_i32 s51, s46, s51\n\t"
        "s_max_i32 s47, s47, s51\n\t"
        "s_lshl_b32 s50, s46, 12\n\t"
        "s_and_b32 s51, s50, 0x1f000\n\t"
        "s_add_u32 m0, s51, %[ring]\n\t"
        "s_and_b32 s50, s50, 0x7000\n\t"
        "s_cbranch_scc0 3f\n\t"
        "s_xor_b32 s52, s53, s50\n\t"
        "s_setpc_b64 s[62:63]\n"
        "3:\n\t"
        "s_lshr_b32 s50, s46, 3\n\t"
        "s_cmp_eq_u32 s50, 64\n\t"
        "s_cbranch_scc1 7f\n\t"
        "v_readlane_b32 s53, %[roff], s50\n\t"
        "v_readfirstlane_b32 s51, %[av]\n\t"
        "v_mov_b32 %[vt], s48\n\t"
        "s_cmp_eq_u32 s50, 32\n\t"
        "s_cselect_b32 s40, s58, s40\n\t"
        "s_cselect_b32 s41, s59, s41\n\t"
        "s_cmp_eq_u32 s50, " STR(ANCHOR) "\n\t"
        "s_cselect_b32 s49, s48, s49\n\t"
        "s_cselect_b32 s57, " STR(GAIN) ", s57\n\t"
        "s_cselect_b32 s54, " STR(NMID) "-" STR(NMIN) ", s54\n\t"
        "s_cmp_lg_u32 s51, 0\n\t"
        "s_cselect_b32 s49, s51, s49\n\t"
        "s_mov_b32 s52, s53\n\t"
        "s_sub_i32 s50, s50, 3\n\t"
        "ds_write_b32 %[vflag], %[vt]\n"
        "6:\n\t"
        "v_cmp_gt_i32 vcc, s50, %[dv]\n\t"
        "ds_read_b32 %[dv], %[dflag]\n\t"
        "ds_read_b32 %[av], %[aflag]\n\t"
        "s_cbranch_vccnz 5f\n"
        "4:\n\t"
        "s_setpc_b64 s[62:63]\n"
        "5:\n\t"
        SPIN_ASM
        "s_waitcnt lgkmcnt(0)\n\t"
        "s_branch 6b\n"
        "7:\n\t"
        "v_mov_b32 %[dv], %[vt]\n\t"
        DRAIN(28) DRAIN(24) DRAIN(20) DRAIN(16) DRAIN(12) DRAIN(8) DRAIN(4) DRAIN(0)
        "s_waitcnt lgkmcnt(0)\n\t"
        "s_endpgm\n"
        "9:"
        : [vt] "=&v"(vt), [dv] "=&v"(dv), [av] "=&v"(av)
        : [voff] "v"(voff), [sco] "v"(sco), [roff] "v"(roff), [flag] "v"(flag), [dflag] "v"(dflag), [aflag] "v"(aflag), [vflag] "v"(vflag),
          [wave] "s"(wave), [ring] "s"(lbase + L_RING), [skew] "s"(bid * SPR * 2 + 4 * PACE),
          [tbo] "s"(((cub & 1016u) | (bid >> 5)) * 2048), [kp] "s"(a.qk), [vp] "s"(a.qv), [ksc] "s"(a.ksc), [kzp] "s"(a.kzp), [vsc] "s"(a.vsc),
          [vzp] "s"(a.vzp), [kcos] "s"(a.kcos), [ksin] "s"(a.ksin), [lsc] "n"(L_SC - L_RING), [n1] "n"(map_mask('6') * 16),
          [n2] "n"(map_mask('7') * 16), [t1] "n"(map_mask('3') * 2048), [t2] "n"(map_mask('4') * 2048)
        : "memory", "vcc", "scc", "s40", "s41", "s42", "s43", "s44", "s45", "s46", "s47", "s48", "s49", "s50", "s51", "s52", "s53",
          "s54", "s55", "s56", "s57", "s58", "s59", "s60", "s61", "s62", "s63");
  }

  const int r = lane >> 3, c = lane & 7, mem = bid & 31;
  const auto wsr = __builtin_amdgcn_make_buffer_rsrc((void*)a.ws, 0, WS_BYTES, 0x00020000);

  if (wave == NC + 1) {
    const u32x4 wsd = {(unsigned)(uint64_t)a.ws, (unsigned)((uint64_t)a.ws >> 32) & 0xffffu, WS_BYTES, 0x20000u};
    const bool sampled = ((bid >> 3) & (256 / XN - 1)) == 3;
    const unsigned exw = WS_EX + ((bid & 7) + (bid / (2048 / XN)) * 8) * 256;
    const unsigned ino = WS_IN + (bid * 32 + r) * 128 + c * 16;
    const unsigned mlr = WS_ML + ((bid & ~31) + (lane & 31)) * 128 + (mem >> 2) * 16 + (lane >> 5) * 8;
    const unsigned mlw = c == 0 ? WS_ML + bid * 128 + r * 16 : WS_BYTES;
    u32x4 snap[4];
#pragma unroll
    for (int i = 0; i < 4; ++i) snap[i] = __builtin_amdgcn_raw_buffer_load_b128(wsr, ino + i * 1024, 0, SC1);
    unsigned mls = __builtin_amdgcn_raw_buffer_load_b32(wsr, mlr + 4, 0, SC1);
    u32x4 mine = __builtin_amdgcn_raw_buffer_load_b128(wsr, mlw, 0, SC1);
    unsigned touch = __builtin_amdgcn_raw_buffer_load_b32(wsr, sampled && lane < NEPOCH ? exw + lane * 8192 : WS_BYTES, 0, SC1);
    const int head = (cub ^ map_bits<3>(mem >> 2, '5')) >> 10;
    const auto outr = __builtin_amdgcn_make_buffer_rsrc((void*)a.out, 0, NHEAD * DH * 2, 0x00020000);
    const unsigned outo = (lane & 8) == 0 ? (head * DH + (mem & 3) * 32 + 4 * c + (lane >> 4)) * 2 : NHEAD * DH * 2;
    if (OUTPF) touch ^= __builtin_amdgcn_raw_buffer_load_b16(outr, outo, 0, OUTPF == 1 ? 0 : SC1);
    asm volatile("s_waitcnt vmcnt(0)" : "+v"(snap[0]), "+v"(snap[1]), "+v"(snap[2]), "+v"(snap[3]), "+v"(mls), "+v"(mine), "+v"(touch)::"memory");
    float wt[4] = {0.f, 0.f, 0.f, 0.f};
    auto post = [&]() {
      mine[1] += 1; mine[3] += 1;
      __builtin_amdgcn_raw_buffer_store_b128(mine, wsr, mlw, 0, SC1);
      asm volatile("" ::: "memory");
    };
#pragma unroll 1
    for (int e = 0; e < NEPOCH; ++e) {
      const int x = EP0 + e * EPS;
      wait_flag(lbase + L_LAND, x);
      const unsigned v = lds_get(lbase + L_VS);
      if (REANCHOR) __builtin_amdgcn_raw_buffer_store_b32(v, wsr, sampled && lane == 0 ? exw + e * 8192 : WS_BYTES, 0, SC1);
      if (e == E_POST + ((bid >> 7) & MLST)) {
        if (MLST) wait_flag(lbase + L_LAND, x + ((bid >> 3) & 15));
        const float m = __uint_as_float(lds_get(lbase + L_MX + lane * 4)), l = __uint_as_float(lds_get(lbase + L_SM + lane * 4));
        const float M = max8(m), Ls = sum8(l * __builtin_amdgcn_exp2f(m - M));
        mine[0] = __float_as_uint(M); mine[2] = __float_as_uint(Ls);
        post();
      }
      if (e == E_ARM + MLST + ((bid >> 7) & ARST)) {
        if (ARST) wait_flag(lbase + L_LAND, x + ((bid >> 3) & 15));
        u32x2 g;
#pragma unroll 1
        for (int it = 1;; ++it) {
          g = __builtin_amdgcn_raw_buffer_load_b64(wsr, mlr, 0, SC1);
          asm volatile("" : "+v"(g)::"memory");
          if (__builtin_amdgcn_ballot_w64(g[1] == mls) == 0) break;
          if (it > SPIN_CAP) { g[0] = 0x7fc00000u; break; }
          __builtin_amdgcn_s_sleep(64);
          if ((it & 7) == 0) post();
        }
        const auto sw = __builtin_amdgcn_permlane32_swap(g[0], g[0], false, false);
        const float Mj = __uint_as_float(sw[0]), Lj = __uint_as_float(sw[1]);
        float M = max8(Mj);
        M = fmaxf(M, dpp<ROR8>(M));
        const auto s16 = __builtin_amdgcn_permlane16_swap(__float_as_uint(M), __float_as_uint(M), false, false);
        M = fmaxf(__uint_as_float(s16[0]), __uint_as_float(s16[1]));
        const float ej = __builtin_amdgcn_exp2f(Mj - M);
        float Lt = sum8(ej * Lj);
        Lt += dpp<ROR8>(Lt);
        Lt = swap16_add(Lt, Lt);
        const int wj = __builtin_bit_cast(int, ej / Lt);
#pragma unroll
        for (int i = 0; i < 4; ++i) wt[i] = __builtin_bit_cast(float, __builtin_amdgcn_ds_bpermute((8 * i + r) * 4, wj));
        lds_put(lbase + L_FLAG + 12, 1u);
      }
      if (REANCHOR == 1) {
        wait_flag(lbase + L_LAND, x + EPD + ((bid >> 3) & EXST));
        unsigned y = 0;
#pragma unroll
        for (int k = 0; k < XN; k += 8) {
          unsigned q[8];
          asm volatile("s_load_dword %0, %8, %9\n\ts_load_dword %1, %8, %9+256\n\ts_load_dword %2, %8, %9+512\n\ts_load_dword %3, %8, %9+768\n\t"
                       "s_load_dword %4, %8, %9+1024\n\ts_load_dword %5, %8, %9+1280\n\ts_load_dword %6, %8, %9+1536\n\t"
                       "s_load_dword %7, %8, %9+1792\n\ts_waitcnt lgkmcnt(0)"
                       : "=&s"(q[0]), "=&s"(q[1]), "=&s"(q[2]), "=&s"(q[3]), "=&s"(q[4]), "=&s"(q[5]), "=&s"(q[6]), "=&s"(q[7])
                       : "s"(a.ws + WS_EX + e * 8192), "n"(k * 256) : "memory");
#pragma unroll
          for (int j = 0; j < 8; ++j) y = (lane & (XN - 1)) == k + j ? q[j] : y;
        }
        const int d = (int)(y - v);
        const float dd = d > -3000 && d < 500 ? (float)d : (float)(EPD * 13);
        float f = dd + dpp<ROR8>(dd);
        f = 0.25f * swap16_add(f, f);
        const float mx = max8(f), mean = 0.125f * sum8(f);
        const unsigned anchor = v + (unsigned)(int)__builtin_floorf(mean + fminf(fmaxf(mx - mean - MXD, 0.f) * (MXW / 16.f), MXC) + 0.5f);
        lds_put(lbase + L_FLAG + 8, anchor ? anchor : 1u);
      }
    }
    wait_flag(lbase + L_LAND, ROUNDS * 8);
    RSTAMP(g0);
    if (mem >> 2 == NB - 1) __builtin_amdgcn_s_sleep(GDELAY); else __builtin_amdgcn_s_sleep(GDELAY0);
    u32x4 xv[4] = {snap[0], snap[1], snap[2], snap[3]}, dx[4];
#pragma unroll 1
    for (int it = 1;; ++it) {
      unsigned left, t0;
      asm volatile(
          "s_movk_i32 %[left], 256\n"
          "1:\n\t"
          GCHK(0, 40) GCHK(1, 42) GCHK(2, 44) GCHK(3, 46)
          "s_or_b64 s[48:49], s[40:41], s[42:43]\n\t"
          "s_or_b64 s[50:51], s[44:45], s[46:47]\n\t"
          "s_or_b64 s[48:49], s[48:49], s[50:51]\n\t"
          "s_cbranch_scc0 2f\n\t"
          GLD(0, 40, 0) GLD(1, 42, 1024) GLD(2, 44, 2048) GLD(3, 46, 3072)
          "s_mov_b64 exec, -1\n\t"
          "s_waitcnt vmcnt(" STR(GD) "*4)\n\t"
          "s_sleep " STR(GSLEEP) "\n\t"
          "s_sub_u32 %[left], %[left], 1\n\t"
          "s_cmp_lg_u32 %[left], 0\n\t"
          "s_cbranch_scc1 1b\n\t"
          "s_waitcnt vmcnt(0)\n"
          "2:"
          : [left] "=&s"(left), [t0] "=&v"(t0), "+{v[64:67]}"(xv[0]), "+{v[68:71]}"(xv[1]), "+{v[72:75]}"(xv[2]), "+{v[76:79]}"(xv[3]),
            "=&{v[96:99]}"(dx[0]), "=&{v[100:103]}"(dx[1]), "=&{v[104:107]}"(dx[2]), "=&{v[108:111]}"(dx[3])
          : [ino] "v"(ino), [rs] "s"(wsd), "{v[80:83]}"(snap[0]), "{v[84:87]}"(snap[1]), "{v[88:91]}"(snap[2]), "{v[92:95]}"(snap[3])
          : "memory", "scc", "s40", "s41", "s42", "s43", "s44", "s45", "s46", "s47", "s48", "s49", "s50", "s51");
      if (__builtin_amdgcn_readfirstlane(left) != 0) break;
      if (it > SPIN_CAP / 64) { wt[0] = __uint_as_float(0x7fc00000u); break; }
      post();
    }
    RSTAMP(g1);
    float o[4];
#pragma unroll
    for (int j = 0; j < 4; ++j) {
      o[j] = wt[0] * __uint_as_float((dx[0][j] ^ snap[0][j]) & ~1u);
#pragma unroll
      for (int i = 1; i < 4; ++i) o[j] = fmaf(wt[i], __uint_as_float((dx[i][j] ^ snap[i][j]) & ~1u), o[j]);
    }
    float u = swap16_add(swap32_add(o[0], o[2]), swap32_add(o[1], o[3]));
    u += dpp<ROR8>(u);
    __builtin_amdgcn_raw_buffer_store_b16(f2bf(u), outr, outo, 0, OUTAUX);
    RSTAMP(g2);
    asm volatile("s_waitcnt vmcnt(0)" ::"{v[64:67]}"(xv[0]), "{v[68:71]}"(xv[1]), "{v[72:75]}"(xv[2]), "{v[76:79]}"(xv[3]) : "memory");
    PUT(8, g0); PUT(9, g1); PUT(10, g2);
    return;
  }

  const int w = wave - 1;
  const int myhead = (cub ^ map_bits<3>(w, '5')) >> 10;
  const unsigned flag = lbase + L_LAND + w * 4, myflag = lbase + L_FLAG + 64 + w * 4;
  if (QDELAY) wait_flag(flag, QDELAY);
  const us q0 = a.q[myhead * DH + lane], q1 = a.q[myhead * DH + 64 + lane];
  const us qc0 = a.qcos[lane], qc1 = a.qcos[64 + lane], qs0 = a.qsin[lane], qs1 = a.qsin[64 + lane];
  const unsigned sendo = lane < 32 ? WS_IN + (((bid & ~31) + 4 * w + r) * 32 + mem) * 128 + c * 16 : WS_BYTES;
  u32x4 old = __builtin_amdgcn_raw_buffer_load_b128(wsr, sendo, 0, SC1);
  const unsigned tabo = (((cub ^ w) & 1023) * 8 + r) * DH + 8 * c;
  if (TDELAY) wait_flag(flag, TDELAY + w * TSTEP);
  u32x4 cl = *reinterpret_cast<const u32x4*>(a.kcos + tabo), ch = *reinterpret_cast<const u32x4*>(a.kcos + tabo + 64);
  u32x4 sl = *reinterpret_cast<const u32x4*>(a.ksin + tabo), sh = *reinterpret_cast<const u32x4*>(a.ksin + tabo + 64);
  {
    const unsigned r0 = f2bf(rbf(bf2f(q0) * bf2f(qc0)) + rbf(-bf2f(q1) * bf2f(qs0)));
    const unsigned r1 = f2bf(rbf(bf2f(q1) * bf2f(qc1)) + rbf(bf2f(q0) * bf2f(qs1)));
    lds_put(lbase + L_QP + w * 512 + lane * 4, r0 | (r1 << 16));
    lds_put(lbase + L_QP + w * 512 + 256 + lane * 4, r1 | ((r0 ^ 0x8000u) << 16));
    asm volatile("s_waitcnt lgkmcnt(0)" ::: "memory");
    if (lane == 0) asm volatile("ds_add_u32 %0, %1" ::"v"(lbase + L_FLAG + 4), "v"(1u) : "memory");
  }
  old = ~old & 1u;
  asm volatile("" : "+v"(old));
  wait_flag(lbase + L_FLAG + 4, NC);
  wait_flag(flag, 5);

  const LAS unsigned char* blk = L + L_RING + w * 4096 + r * 512 + c * 32;
  const LAS us* sc = reinterpret_cast<const LAS us*>(L + L_SC) + ((cub ^ w) & 7) * 8 + r;
  constexpr float C2 = 0.088388347648318440f * 1.4426950408889634f;
  float S[NB];
#pragma unroll
  for (int n = 0; n < NB; ++n) S[n] = 0.f;

#pragma unroll 1
  for (int t = 0; t < 4; ++t) {
    unsigned tab[16];
#pragma unroll
    for (int i = 0; i < 4; ++i) {
      tab[2 * i] = __builtin_amdgcn_perm(sh[i], cl[i], 0x05040100u);
      tab[2 * i + 1] = __builtin_amdgcn_perm(sh[i], cl[i], 0x07060302u);
      tab[8 + 2 * i] = __builtin_amdgcn_perm(sl[i], ch[i], 0x05040100u);
      tab[8 + 2 * i + 1] = __builtin_amdgcn_perm(sl[i], ch[i], 0x07060302u);
    }
#pragma unroll
    for (int i = 0; i < 16; ++i) asm volatile("" : "+v"(tab[i]));
    const unsigned tnext = tabo ^ (map_bits<2>(t + 1, '3') * 8 * DH);
#pragma unroll 1
    for (int n = 0; n < NB; ++n) {
      const int rho = t * 8 + n;
      const LAS u32x4* qpp = reinterpret_cast<const LAS u32x4*>(L + L_QP + n * 512 + c * 32);
      const u32x4 qp[4] = {qpp[0], qpp[1], qpp[16], qpp[17]};
      float wv[16];
#pragma unroll
      for (int e = 0; e < 16; ++e) wv[e] = dot2(tab[e], qp[e >> 2][e & 3]);
      const float sb = sum8(((wv[0] + wv[1]) + (wv[2] + wv[3])) + ((wv[4] + wv[5]) + (wv[6] + wv[7])) +
                            (((wv[8] + wv[9]) + (wv[10] + wv[11])) + ((wv[12] + wv[13]) + (wv[14] + wv[15]))));
      wait_flag(flag, rho * 8 + w + 1);
      const LAS i32x4* p = reinterpret_cast<const LAS i32x4*>(blk + (rho & 3) * 32768);
      i32x4 x[4] = {p[0], p[1], p[16], p[17]};
      const float ks = bf2f(sc[n * 256 + t * 64]), kz = bf2f(sc[2048 + n * 256 + t * 64]);
      asm volatile("s_waitcnt lgkmcnt(0)" : "+v"(x[0]), "+v"(x[1]), "+v"(x[2]), "+v"(x[3])::"memory");
      lds_put(myflag, rho + 1);
      if (t < 3) {
        if (n == 3) cl = *reinterpret_cast<const u32x4*>(a.kcos + tnext);
        if (n == 4) ch = *reinterpret_cast<const u32x4*>(a.kcos + tnext + 64);
        if (n == 5) sl = *reinterpret_cast<const u32x4*>(a.ksin + tnext);
        if (n == 6) sh = *reinterpret_cast<const u32x4*>(a.ksin + tnext + 64);
      }
      float a0 = 0.f, a1 = 0.f, a2 = 0.f, a3 = 0.f;
#pragma unroll
      for (int j = 0; j < 4; ++j) {
        a0 = fmaf((float)x[j][0], wv[4 * j], a0); a1 = fmaf((float)x[j][1], wv[4 * j + 1], a1);
        a2 = fmaf((float)x[j][2], wv[4 * j + 2], a2); a3 = fmaf((float)x[j][3], wv[4 * j + 3], a3);
      }
      const float s = C2 * ks * (sum8((a0 + a1) + (a2 + a3)) - kz * sb);
      float keep = (c & 3) == t ? s : S[0];
      if (t == 3) {
        const float m = fmaxf(max32(keep), -3.0e38f);
        keep = __builtin_amdgcn_exp2f(keep - m);
        const float l = sum32(keep);
        if (lane < 2) lds_put(lbase + (lane ? L_SM : L_MX) + (n * NC + w) * 4, __float_as_uint(lane ? l : m));
      }
#pragma unroll
      for (int e = 0; e < NB - 1; ++e) S[e] = S[e + 1];
      S[NB - 1] = keep;
    }
  }

  const LAS unsigned char* vblk = L + L_RING + w * 4096 + lane * 16;
  LAS unsigned char* stage = L + L_RING + w * 4096 + (lane & 31) * 16 + (lane >> 5) * 8;
  const LAS unsigned char* gath = L + L_RING + (lane >> 5) * 16384 + w * 512 + (lane & 31) * 16;
  auto merge_send = [&](unsigned cnt, int need) {
    f32x4 o[4];
    SPIN_DECL;
    do {
      unsigned k;
      asm volatile("ds_read_b32 %0, %5\n\tds_read_b128 %1, %6\n\tds_read_b128 %2, %6 offset:4096\n\tds_read_b128 %3, %6 offset:8192\n\t"
                   "ds_read_b128 %4, %6 offset:12288\n\ts_waitcnt lgkmcnt(0)"
                   : "=&v"(k), "=&v"(o[0]), "=&v"(o[1]), "=&v"(o[2]), "=&v"(o[3]) : "v"(cnt), "v"((unsigned)(uintptr_t)gath) : "memory");
      if (__builtin_amdgcn_ballot_w64((int)k < need) == 0) break;
    } while (true SPIN_OK);
    f32x4 tot = (o[0] + o[1]) + (o[2] + o[3]);
#pragma unroll
    for (int j = 0; j < 4; ++j) tot[j] = swap32_add(tot[j], tot[j]);
    __builtin_amdgcn_raw_buffer_store_b128((__builtin_bit_cast(u32x4, tot) & ~1u) | old, wsr, sendo, 0, SENDAUX);
  };
  f32x2 po[NB - 1], last = {0.f, 0.f};
#pragma unroll
  for (int n = 0; n < NB - 1; ++n) po[n] = f32x2{0.f, 0.f};
#pragma unroll 1
  for (int n = 0; n < NB; ++n) {
    const float pe = S[0];
#pragma unroll
    for (int e = 0; e < NB - 1; ++e) S[e] = S[e + 1];
#pragma unroll
    for (int e = 0; e < NB - 2; ++e) po[e] = po[e + 1];
    po[NB - 2] = last;
    wait_flag(flag, (32 + n * 4) * 8 + w + 1);
    const float vs = bf2f(sc[4096 + n * 256 + (c & 3) * 64]), vz = bf2f(sc[6144 + n * 256 + (c & 3) * 64]);
    const LAS float* mx = reinterpret_cast<const LAS float*>(L + L_MX) + n * NC;
    const float P = pe * vs * __builtin_amdgcn_exp2f(mx[w] - max8(mx[c]));
    const float corr = 0.5f * sum32(P * vz);
    f32x4 acc = {-corr, -corr, -corr, -corr};
#pragma unroll
    for (int t = 0; t < 4; ++t) {
      const int rho = 32 + n * 4 + t;
      float pt[4];
#pragma unroll
      for (int i = 0; i < 4; ++i)
        pt[i] = __builtin_bit_cast(float, __builtin_amdgcn_ds_bpermute((16 * i + 8 * (lane >> 5) + t) * 4, __builtin_bit_cast(int, P)));
      asm volatile("" : "+v"(pt[0]), "+v"(pt[1]), "+v"(pt[2]), "+v"(pt[3]));
      if (t == 3 && n == NB - 1) {
        wait_flag(lbase + L_FLAG + 12, 1);
        if (EARLY && w != NC - 1) merge_send(lbase + L_FLAG + 64 + c * 4, ROUNDS - 2);
      }
      if (t) wait_flag(flag, rho * 8 + w + 1);
      const LAS i32x4* p = reinterpret_cast<const LAS i32x4*>(vblk + t * 32768);
      i32x4 x[4] = {p[0], p[64], p[128], p[192]};
      if (t == 3) {
#pragma unroll
        for (int i = 0; i < 4; ++i)
#pragma unroll
          for (int j = 0; j < 4; ++j) acc[j] = fmaf((float)x[i][j], pt[i], acc[j]);
        last = f32x2{swap32_add(acc[0], acc[2]), swap32_add(acc[1], acc[3])};
        asm volatile("" : "+v"(last)::"memory");
        if (n == NB - 1) *reinterpret_cast<LAS f32x2*>(stage + (NB - 1) * 512) = last;
        lds_put(myflag, rho + 1);
      } else {
        asm volatile("s_waitcnt lgkmcnt(0)" : "+v"(x[0]), "+v"(x[1]), "+v"(x[2]), "+v"(x[3])::"memory");
        lds_put(myflag, rho + 1);
        if (t == 0 && n == NB - 1) {
#pragma unroll
          for (int e = 0; e < NB - 1; ++e) *reinterpret_cast<LAS f32x2*>(stage + e * 512) = po[e];
        }
#pragma unroll
        for (int i = 0; i < 4; ++i)
#pragma unroll
          for (int j = 0; j < 4; ++j) acc[j] = fmaf((float)x[i][j], pt[i], acc[j]);
        asm volatile("" : "+v"(acc));
      }
    }
  }
  CSTAMP(r9);
  if (!EARLY || w == NC - 1) merge_send(lbase + L_FLAG + 64 + c * 4, w == NC - 1 ? ROUNDS : ROUNDS - 2);
  CSTAMP(r10);
  CSTAMP(r11);
  CPUT(8, r9); CPUT(9, r10); CPUT(10, r11);
}

}

extern "C" void kernel_launch(void* const* d_in, const int* in_sizes, int n_in, void* d_out, int out_size, void* d_ws,
                              size_t ws_size, hipStream_t stream) {
  Args a;
  a.q = (const us*)d_in[0]; a.qk = (const int*)d_in[1]; a.ksc = (const us*)d_in[2]; a.kzp = (const us*)d_in[3];
  a.qv = (const int*)d_in[4]; a.vsc = (const us*)d_in[5]; a.vzp = (const us*)d_in[6]; a.qcos = (const us*)d_in[7];
  a.qsin = (const us*)d_in[8]; a.kcos = (const us*)d_in[9]; a.ksin = (const us*)d_in[10];
  a.ws = (unsigned char*)d_ws; a.out = (us*)d_out;
  attn_fused<<<NWG, NTHR, 0, stream>>>(a);
}
```
